```python
import torch

def _softmax(x, axis=-1):
    return torch.softmax(x, dim=axis)
def _gelu(x):
    return torch.nn.functional.gelu(x, approximate="tanh")
def _silu(x):
    return torch.nn.functional.silu(x)
def _sigmoid(x):
    return torch.sigmoid(x)
def _layernorm(x, axis=-1, eps=1e-5):
    m = x.mean(dim=axis, keepdim=True)
    v = x.var(dim=axis, keepdim=True, unbiased=False)
    return (x - m) / torch.sqrt(v + eps)
def _scatter_add(tgt, idx, src, axis=0):
    return tgt.clone().index_add_(axis, idx, src)
def _segment_sum(src, seg, n):
    out = torch.zeros((n,) + src.shape[1:], dtype=src.dtype)
    return out.index_add_(0, seg, src)
def _pad(x, pw):
    flat = tuple(v for pair in reversed(pw) for v in pair)
    return torch.nn.functional.pad(x, flat)

kernel_name = 'standalone__gqa_block_cached__near_ridge'

def setup_inputs():

    _w = (lambda a: (lambda t: t.to(torch.bfloat16) if t.is_floating_point() else t)(torch.as_tensor(a)))
    B, Sq, D = 16, 80, 512
    Hq, Hkv, Sk, Dh = 32, 8, 1024, 128
    assert Hq % Hkv == 0, (Hq, Hkv)
    return {
        "x": _w((lambda *s: torch.randn(*s, dtype=torch.bfloat16) * 0.8)(B, Sq, D)),
        "k_cache": _w((lambda *s: torch.randn(*s, dtype=torch.bfloat16) * 0.8)(B, Hkv, Sk, Dh)),
        "v_cache": _w((lambda *s: torch.randn(*s, dtype=torch.bfloat16) * 0.05)(B, Hkv, Sk, Dh)),
        "wq": _w((lambda *s: torch.randn(*s, dtype=torch.bfloat16) * 0.8)(D, Hq * Dh) * (D ** -0.5)),
        "wo": _w((lambda *s: torch.randn(*s, dtype=torch.bfloat16) * 0.05)(Hq * Dh, D) * ((Hq * Dh) ** -0.5)),
    }

def reference(x, k_cache, v_cache, wq, wo):
    B, Sq, D = x.shape
    Hq, Dh = 32, 128
    Hkv = k_cache.shape[1]
    rep = Hq // Hkv
    q = (lambda x, *d: x.permute(*d))((x @ wq).reshape(B, Sq, Hq, Dh), 0, 2, 1, 3)
    k = (lambda x, n, axis: torch.repeat_interleave(x, n, dim=axis))(k_cache, rep, axis=1)
    v = (lambda x, n, axis: torch.repeat_interleave(x, n, dim=axis))(v_cache, rep, axis=1)
    scores = torch.einsum("bhqd,bhkd->bhqk", q, k) * (Dh ** -0.5)
    p = _softmax(scores, axis=-1)
    o = torch.einsum("bhqk,bhkd->bhqd", p, v)
    return (lambda x, *d: x.permute(*d))(o, 0, 2, 1, 3).reshape(B, Sq, Hq * Dh) @ wo
```

```cpp
#include <hip/hip_runtime.h>
#include <hip/hip_bf16.h>

using bf16 = __hip_bfloat16;
using bf16x8 = __attribute__((ext_vector_type(8))) short;
using s16x4 = __attribute__((ext_vector_type(4))) short;
using f32x4 = __attribute__((ext_vector_type(4))) float;
using u32x2 = __attribute__((ext_vector_type(2))) unsigned;
typedef __attribute__((address_space(3))) s16x4 lds_s16x4;

constexpr int NB = 16, SQ = 80, DM = 512, HQ = 32, HKV = 8, SK = 1024, DH = 128;
constexpr int MROWS = NB * SQ;
constexpr int QCOLS = HQ * DH;
constexpr float CEXP = 0.088388347648318440f * 1.4426950408889634f;

__device__ __forceinline__ unsigned cvtpk(float lo, float hi) { unsigned r; asm volatile("v_cvt_pk_bf16_f32 %0, %1, %2" : "=v"(r) : "v"(lo), "v"(hi)); return r; }
__device__ __forceinline__ unsigned offb(unsigned row, unsigned ch) { return 256u * row + 16u * (ch ^ (((row & 3) << 2) | ((row >> 2) & 3))); }
__device__ __forceinline__ s16x4 trread(const char* p) { return __builtin_amdgcn_ds_read_tr16_b64_v4i16((lds_s16x4*)p); }
__device__ __forceinline__ float xor16max(float v) { auto r = __builtin_amdgcn_permlane16_swap(__float_as_uint(v), __float_as_uint(v), false, false); return fmaxf(__uint_as_float(r[0]), __uint_as_float(r[1])); }
__device__ __forceinline__ float xor32max(float v) { auto r = __builtin_amdgcn_permlane32_swap(__float_as_uint(v), __float_as_uint(v), false, false); return fmaxf(__uint_as_float(r[0]), __uint_as_float(r[1])); }
__device__ __forceinline__ float xor16add(float v) { auto r = __builtin_amdgcn_permlane16_swap(__float_as_uint(v), __float_as_uint(v), false, false); return __uint_as_float(r[0]) + __uint_as_float(r[1]); }
__device__ __forceinline__ float xor32add(float v) { auto r = __builtin_amdgcn_permlane32_swap(__float_as_uint(v), __float_as_uint(v), false, false); return __uint_as_float(r[0]) + __uint_as_float(r[1]); }

template <int BM, int WGM, int WGN, bool OUTF32>
__global__ __launch_bounds__(256) void gemm_kernel(const bf16* __restrict__ A, const bf16* __restrict__ Bm, void* __restrict__ Cout,
                                                   int M, int N, int K, int ksplit_len) {
  constexpr int BN = 128, MT = BM / 16 / WGM, NT = BN / 16 / WGN;
  constexpr int ABYTES = BM * 128, BBYTES = 64 * 256, BUF = ABYTES + BBYTES;
  extern __shared__ __attribute__((aligned(16))) char lds[];
  const int tid = threadIdx.x, wid = tid >> 6, lane = tid & 63, l15 = lane & 15, g = lane >> 4;
  const int n0 = blockIdx.x * BN, m0 = blockIdx.y * BM, kbeg = blockIdx.z * ksplit_len, nk = ksplit_len / 64;
  const int wm = wid / WGN, wn = wid % WGN;
  auto stage = [&](int kt, int buf) {
    const int k0 = kbeg + kt * 64;
    char* base = lds + buf * BUF;
    for (int pc = wid; pc < BM / 8; pc += 4) {
      const int row = pc * 8 + (lane >> 3), ch = (lane & 7) ^ (row & 7);
      __builtin_amdgcn_global_load_lds((const unsigned*)(A + (size_t)(m0 + row) * K + k0 + ch * 8), (unsigned*)(base + pc * 1024), 16, 0, 0);
    }
#pragma unroll
    for (int i = 0; i < 4; ++i) {
      const int pc = wid * 4 + i, row = pc * 4 + (lane >> 4), ch = (lane & 15) ^ (((row & 3) << 2) | ((row >> 2) & 3));
      __builtin_amdgcn_global_load_lds((const unsigned*)(Bm + (size_t)(k0 + row) * N + n0 + ch * 8), (unsigned*)(base + ABYTES + pc * 1024), 16, 0, 0);
    }
  };
  f32x4 acc[NT][MT];
#pragma unroll
  for (int a = 0; a < NT; ++a)
#pragma unroll
    for (int b = 0; b < MT; ++b) acc[a][b] = f32x4{0.f, 0.f, 0.f, 0.f};
  stage(0, 0);
  for (int kt = 0; kt < nk; ++kt) {
    asm volatile("s_waitcnt vmcnt(0)" ::: "memory");
    __syncthreads();
    if (kt + 1 < nk) stage(kt + 1, (kt + 1) & 1);
    const char* Ab = lds + (kt & 1) * BUF;
    const char* Bb = Ab + ABYTES;
#pragma unroll
    for (int s = 0; s < 2; ++s) {
      bf16x8 af[MT], bfr[NT];
#pragma unroll
      for (int mt = 0; mt < MT; ++mt) { const int row = (wm * MT + mt) * 16 + l15; af[mt] = *(const bf16x8*)(Ab + row * 128 + (((4 * s + g) ^ (row & 7)) * 16)); }
#pragma unroll
      for (int nt = 0; nt < NT; ++nt) { const int c = wn * NT + nt;
        s16x4 lo = trread(Bb + offb(32 * s + 8 * g + (l15 >> 2), 2 * c + ((lane & 3) >> 1)) + 8 * (lane & 1));
        s16x4 hi = trread(Bb + offb(32 * s + 8 * g + 4 + (l15 >> 2), 2 * c + ((lane & 3) >> 1)) + 8 * (lane & 1));
        bfr[nt] = bf16x8{lo[0], lo[1], lo[2], lo[3], hi[0], hi[1], hi[2], hi[3]}; }
#pragma unroll
      for (int nt = 0; nt < NT; ++nt)
#pragma unroll
        for (int mt = 0; mt < MT; ++mt) acc[nt][mt] = __builtin_amdgcn_mfma_f32_16x16x32_bf16(bfr[nt], af[mt], acc[nt][mt], 0, 0, 0);
    }
  }
#pragma unroll
  for (int nt = 0; nt < NT; ++nt)
#pragma unroll
    for (int mt = 0; mt < MT; ++mt) {
      const int m = m0 + (wm * MT + mt) * 16 + l15, n = n0 + (wn * NT + nt) * 16 + 4 * g;
      if constexpr (OUTF32) { *(f32x4*)((float*)Cout + (size_t)blockIdx.z * M * N + (size_t)m * N + n) = acc[nt][mt]; }
      else { u32x2 w = {cvtpk(acc[nt][mt][0], acc[nt][mt][1]), cvtpk(acc[nt][mt][2], acc[nt][mt][3])}; *(u32x2*)((bf16*)Cout + (size_t)m * N + n) = w; }
    }
}

__global__ __launch_bounds__(256) void reduce_kernel(const float* __restrict__ slabs, bf16* __restrict__ out, int n, int nsplit) {
  const int i = (blockIdx.x * 256 + threadIdx.x) * 4;
  if (i >= n) return;
  f32x4 a = *(const f32x4*)(slabs + i);
  for (int s = 1; s < nsplit; ++s) { f32x4 b = *(const f32x4*)(slabs + (size_t)s * n + i); a += b; }
  u32x2 w = {cvtpk(a[0], a[1]), cvtpk(a[2], a[3])};
  *(u32x2*)(out + i) = w;
}

template <int NQT>
__device__ __forceinline__ void attn_body(const bf16* __restrict__ Qh, const bf16* __restrict__ Kg, const bf16* __restrict__ Vg, bf16* __restrict__ Oh,
                                          char* lds, int wid, int lane) {
  const int l15 = lane & 15, g = lane >> 4, sh = wid & 1;
  bf16x8 qf[NQT][4];
#pragma unroll
  for (int qt = 0; qt < NQT; ++qt)
#pragma unroll
    for (int ks = 0; ks < 4; ++ks) qf[qt][ks] = *(const bf16x8*)(Qh + (size_t)(qt * 16 + l15) * QCOLS + ks * 32 + g * 8);
  f32x4 O[8][NQT];
#pragma unroll
  for (int dt = 0; dt < 8; ++dt)
#pragma unroll
    for (int qt = 0; qt < NQT; ++qt) O[dt][qt] = f32x4{0.f, 0.f, 0.f, 0.f};
  float mrow[NQT], lrow[NQT];
#pragma unroll
  for (int qt = 0; qt < NQT; ++qt) { mrow[qt] = -__builtin_inff(); lrow[qt] = 0.f; }

  const int st_ = wid >> 2, isV_ = (wid >> 1) & 1;
  const bf16* sbase = (isV_ ? Vg : Kg) + (size_t)st_ * 512 * DH;
  auto stage = [&](int j, int buf) {
    int ln = lane; asm volatile("" : "+v"(ln));
    char* dst = lds + ((buf * 2 + st_) * 2 + isV_) * 16384 + (wid & 1) * 8192;
#pragma unroll
    for (int i = 0; i < 8; ++i) {
      const int row = ((wid & 1) * 8 + i) * 4 + (ln >> 4), ch = (ln & 15) ^ (((row & 3) << 2) | ((row >> 2) & 3));
      const int krow = isV_ ? ((row & ~31) | (((row >> 2) & 1) << 4) | (((row >> 3) & 3) << 2) | (row & 3)) : row;
      __builtin_amdgcn_global_load_lds((const unsigned*)(sbase + (j * 64 + krow) * DH + ch * 8), (unsigned*)(dst + i * 1024), 16, 0, 0);
    }
  };
  stage(0, 0);
  for (int j = 0; j < 8; ++j) {
    asm volatile("s_waitcnt vmcnt(0)" ::: "memory");
    __syncthreads();
    if (j + 1 < 8) stage(j + 1, (j + 1) & 1);
    const char* Kb = lds + (((j & 1) * 2 + sh) * 2) * 16384;
    const char* Vb = Kb + 16384;
    int lv = lane; asm volatile("" : "+v"(lv));
    const int lv15 = lv & 15, gv = lv >> 4;
    f32x4 S[4][NQT];
#pragma unroll
    for (int t = 0; t < 4; ++t)
#pragma unroll
      for (int qt = 0; qt < NQT; ++qt) S[t][qt] = f32x4{0.f, 0.f, 0.f, 0.f};
#pragma unroll
    for (int t = 0; t < 4; ++t)
#pragma unroll
      for (int ks = 0; ks < 4; ++ks) {
        const bf16x8 kf = *(const bf16x8*)(Kb + offb(16 * t + l15, 4 * ks + g));
#pragma unroll
        for (int qt = 0; qt < NQT; ++qt) S[t][qt] = __builtin_amdgcn_mfma_f32_16x16x32_bf16(kf, qf[qt][ks], S[t][qt], 0, 0, 0);
      }
    bf16x8 pf[2][NQT];
#pragma unroll
    for (int qt = 0; qt < NQT; ++qt) {
      float mx = S[0][qt][0];
#pragma unroll
      for (int t = 0; t < 4; ++t)
#pragma unroll
        for (int r = 0; r < 4; ++r) mx = fmaxf(mx, S[t][qt][r]);
      mx = xor32max(xor16max(mx));
      const float mnew = fmaxf(mrow[qt], mx);
      const float alpha = __builtin_amdgcn_exp2f((mrow[qt] - mnew) * CEXP);
      mrow[qt] = mnew;
      const float nm = -mnew * CEXP;
      float ps = 0.f;
#pragma unroll
      for (int t = 0; t < 4; ++t)
#pragma unroll
        for (int r = 0; r < 4; ++r) { const float p = __builtin_amdgcn_exp2f(fmaf(S[t][qt][r], CEXP, nm)); S[t][qt][r] = p; ps += p; }
      lrow[qt] = lrow[qt] * alpha + ps;
#pragma unroll
      for (int dt = 0; dt < 8; ++dt) O[dt][qt] *= alpha;
#pragma unroll
      for (int u = 0; u < 2; ++u) {
        __attribute__((ext_vector_type(4))) unsigned w = {cvtpk(S[2 * u][qt][0], S[2 * u][qt][1]), cvtpk(S[2 * u][qt][2], S[2 * u][qt][3]),
                                                           cvtpk(S[2 * u + 1][qt][0], S[2 * u + 1][qt][1]), cvtpk(S[2 * u + 1][qt][2], S[2 * u + 1][qt][3])};
        pf[u][qt] = *reinterpret_cast<bf16x8*>(&w);
      }
    }
#pragma unroll
    for (int u = 0; u < 2; ++u)
#pragma unroll
      for (int dt = 0; dt < 8; ++dt) {
        s16x4 lo = trread(Vb + offb(32 * u + 8 * gv + (lv15 >> 2), 2 * dt + ((lv & 3) >> 1)) + 8 * (lv & 1));
        s16x4 hi = trread(Vb + offb(32 * u + 8 * gv + 4 + (lv15 >> 2), 2 * dt + ((lv & 3) >> 1)) + 8 * (lv & 1));
        const bf16x8 vf = bf16x8{lo[0], lo[1], lo[2], lo[3], hi[0], hi[1], hi[2], hi[3]};
#pragma unroll
        for (int qt = 0; qt < NQT; ++qt) O[dt][qt] = __builtin_amdgcn_mfma_f32_16x16x32_bf16(vf, pf[u][qt], O[dt][qt], 0, 0, 0);
      }
  }
#pragma unroll
  for (int qt = 0; qt < NQT; ++qt) lrow[qt] = xor32add(xor16add(lrow[qt]));
  __syncthreads();
  char* xb = lds + (wid >> 1) * 32768;
  if (sh == 1) {
#pragma unroll
    for (int dt = 0; dt < 8; ++dt)
#pragma unroll
      for (int qt = 0; qt < NQT; ++qt) *(f32x4*)(xb + ((dt * NQT + qt) * 64 + lane) * 16) = O[dt][qt];
#pragma unroll
    for (int qt = 0; qt < NQT; ++qt) { *(float*)(xb + 28672 + (qt * 64 + lane) * 4) = mrow[qt]; *(float*)(xb + 28672 + 1024 + (qt * 64 + lane) * 4) = lrow[qt]; }
  }
  __syncthreads();
  if (sh == 0) {
#pragma unroll
    for (int qt = 0; qt < NQT; ++qt) {
      const float m2 = *(const float*)(xb + 28672 + (qt * 64 + lane) * 4), l2 = *(const float*)(xb + 28672 + 1024 + (qt * 64 + lane) * 4);
      const float mn = fmaxf(mrow[qt], m2);
      const float a1 = __builtin_amdgcn_exp2f((mrow[qt] - mn) * CEXP), a2 = __builtin_amdgcn_exp2f((m2 - mn) * CEXP);
      const float inv = 1.f / (lrow[qt] * a1 + l2 * a2);
      const float w1 = a1 * inv, w2 = a2 * inv;
#pragma unroll
      for (int dt = 0; dt < 8; ++dt) {
        const f32x4 o2 = *(const f32x4*)(xb + ((dt * NQT + qt) * 64 + lane) * 16);
        const f32x4 o = O[dt][qt] * w1 + o2 * w2;
        u32x2 w = {cvtpk(o[0], o[1]), cvtpk(o[2], o[3])};
        *(u32x2*)(Oh + (size_t)(qt * 16 + l15) * QCOLS + dt * 16 + 4 * g) = w;
      }
    }
  }
}

__global__ __launch_bounds__(512, 2) void attn_kernel(const bf16* __restrict__ Qws, const bf16* __restrict__ Kc, const bf16* __restrict__ Vc, bf16* __restrict__ Ows) {
  extern __shared__ __attribute__((aligned(16))) char lds[];
  const int bid = blockIdx.x, xcd = bid & 7, jj = bid >> 3;
  const int grp = xcd * 16 + (jj >> 1), pr = jj & 1;
  const int b = grp >> 3, hkv = grp & 7;
  const int tid = threadIdx.x, wid = __builtin_amdgcn_readfirstlane(tid >> 6), lane = tid & 63;
  const int h = hkv * 4 + pr * 2 + ((wid & 3) >> 1), part = wid >> 2;
  const bf16* Kg = Kc + (size_t)grp * SK * DH;
  const bf16* Vg = Vc + (size_t)grp * SK * DH;
  const size_t qoff = (size_t)(b * SQ + part * 48) * QCOLS + h * DH;
  if (part == 0) attn_body<3>(Qws + qoff, Kg, Vg, Ows + qoff, lds, wid, lane);
  else attn_body<2>(Qws + qoff, Kg, Vg, Ows + qoff, lds, wid, lane);
}

extern "C" void kernel_launch(void* const* d_in, const int* in_sizes, int n_in, void* d_out, int out_size, void* d_ws, size_t ws_size, hipStream_t stream) {
  const bf16* x = (const bf16*)d_in[0];
  const bf16* kc = (const bf16*)d_in[1];
  const bf16* vc = (const bf16*)d_in[2];
  const bf16* wq = (const bf16*)d_in[3];
  const bf16* wo = (const bf16*)d_in[4];
  bf16* out = (bf16*)d_out;
  char* ws = (char*)d_ws;
  bf16* Qws = (bf16*)ws;
  bf16* Ows = (bf16*)(ws + (size_t)MROWS * QCOLS * 2);
  float* slabs = (float*)(ws + (size_t)MROWS * QCOLS * 4);
  constexpr int G1LDS = 2 * (128 * 128 + 64 * 256), G2LDS = 2 * (80 * 128 + 64 * 256), ALDS = 131072;
  hipFuncSetAttribute((const void*)gemm_kernel<128, 2, 2, false>, hipFuncAttributeMaxDynamicSharedMemorySize, G1LDS);
  hipFuncSetAttribute((const void*)gemm_kernel<80, 1, 4, true>, hipFuncAttributeMaxDynamicSharedMemorySize, G2LDS);
  hipFuncSetAttribute((const void*)attn_kernel, hipFuncAttributeMaxDynamicSharedMemorySize, ALDS);
  hipLaunchKernelGGL((gemm_kernel<128, 2, 2, false>), dim3(QCOLS / 128, MROWS / 128, 1), dim3(256), G1LDS, stream, x, wq, (void*)Qws, MROWS, QCOLS, DM, DM);
  hipLaunchKernelGGL(attn_kernel, dim3(256), dim3(512), ALDS, stream, Qws, kc, vc, Ows);
  hipLaunchKernelGGL((gemm_kernel<80, 1, 4, true>), dim3(DM / 128, MROWS / 80, 4), dim3(256), G2LDS, stream, Ows, wo, (void*)slabs, MROWS, DM, QCOLS, QCOLS / 4);
  hipLaunchKernelGGL(reduce_kernel, dim3(MROWS * DM / 4 / 256), dim3(256), 0, stream, slabs, out, MROWS * DM, 4);
}
```

```cpp
#include <hip/hip_runtime.h>
#include <hip/hip_bf16.h>

using bf16 = __hip_bfloat16;
using bf16x8 = __attribute__((ext_vector_type(8))) short;
using s16x4 = __attribute__((ext_vector_type(4))) short;
using f32x4 = __attribute__((ext_vector_type(4))) float;
using u32x2 = __attribute__((ext_vector_type(2))) unsigned;
typedef __attribute__((address_space(3))) s16x4 lds_s16x4;

constexpr int NB = 16, SQ = 80, DM = 512, HQ = 32, HKV = 8, SK = 1024, DH = 128;
constexpr int MROWS = NB * SQ;
constexpr int QCOLS = HQ * DH;
constexpr float CEXP = 0.088388347648318440f * 1.4426950408889634f;

__device__ __forceinline__ unsigned cvtpk(float lo, float hi) { unsigned r; asm volatile("v_cvt_pk_bf16_f32 %0, %1, %2" : "=v"(r) : "v"(lo), "v"(hi)); return r; }
__device__ __forceinline__ unsigned offb(unsigned row, unsigned ch) { return 256u * row + 16u * (ch ^ (((row & 3) << 2) | ((row >> 2) & 3))); }
__device__ __forceinline__ s16x4 trread(const char* p) { s16x4 r; asm volatile("ds_read_b64_tr_b16 %0, %1\n\ts_waitcnt lgkmcnt(0)" : "=&v"(r) : "v"((unsigned)(size_t)p) : "memory"); return r; }
__device__ __forceinline__ float xor16max(float v) { auto r = __builtin_amdgcn_permlane16_swap(__float_as_uint(v), __float_as_uint(v), false, false); return fmaxf(__uint_as_float(r[0]), __uint_as_float(r[1])); }
__device__ __forceinline__ float xor32max(float v) { auto r = __builtin_amdgcn_permlane32_swap(__float_as_uint(v), __float_as_uint(v), false, false); return fmaxf(__uint_as_float(r[0]), __uint_as_float(r[1])); }
__device__ __forceinline__ float xor16add(float v) { auto r = __builtin_amdgcn_permlane16_swap(__float_as_uint(v), __float_as_uint(v), false, false); return __uint_as_float(r[0]) + __uint_as_float(r[1]); }
__device__ __forceinline__ float xor32add(float v) { auto r = __builtin_amdgcn_permlane32_swap(__float_as_uint(v), __float_as_uint(v), false, false); return __uint_as_float(r[0]) + __uint_as_float(r[1]); }

template <int BM, int WGM, int WGN, bool OUTF32>
__global__ __launch_bounds__(256) void gemm_kernel(const bf16* __restrict__ A, const bf16* __restrict__ Bm, void* __restrict__ Cout,
                                                   int M, int N, int K, int ksplit_len) {
  constexpr int BN = 128, MT = BM / 16 / WGM, NT = BN / 16 / WGN;
  constexpr int ABYTES = BM * 128, BBYTES = 64 * 256, BUF = ABYTES + BBYTES;
  extern __shared__ __attribute__((aligned(16))) char lds[];
  const int tid = threadIdx.x, wid = tid >> 6, lane = tid & 63, l15 = lane & 15, g = lane >> 4;
  const int n0 = blockIdx.x * BN, m0 = blockIdx.y * BM, kbeg = blockIdx.z * ksplit_len, nk = ksplit_len / 64;
  const int wm = wid / WGN, wn = wid % WGN;
  auto stage = [&](int kt, int buf) {
    const int k0 = kbeg + kt * 64;
    char* base = lds + buf * BUF;
    for (int pc = wid; pc < BM / 8; pc += 4) {
      const int row = pc * 8 + (lane >> 3), ch = (lane & 7) ^ (row & 7);
      __builtin_amdgcn_global_load_lds((const unsigned*)(A + (size_t)(m0 + row) * K + k0 + ch * 8), (unsigned*)(base + pc * 1024), 16, 0, 0);
    }
#pragma unroll
    for (int i = 0; i < 4; ++i) {
      const int pc = wid * 4 + i, row = pc * 4 + (lane >> 4), ch = (lane & 15) ^ (((row & 3) << 2) | ((row >> 2) & 3));
      __builtin_amdgcn_global_load_lds((const unsigned*)(Bm + (size_t)(k0 + row) * N + n0 + ch * 8), (unsigned*)(base + ABYTES + pc * 1024), 16, 0, 0);
    }
  };
  f32x4 acc[NT][MT];
#pragma unroll
  for (int a = 0; a < NT; ++a)
#pragma unroll
    for (int b = 0; b < MT; ++b) acc[a][b] = f32x4{0.f, 0.f, 0.f, 0.f};
  stage(0, 0);
  for (int kt = 0; kt < nk; ++kt) {
    asm volatile("s_waitcnt vmcnt(0)" ::: "memory");
    __syncthreads(); __builtin_amdgcn_sched_barrier(0);
    if (kt + 1 < nk) stage(kt + 1, (kt + 1) & 1);
    const char* Ab = lds + (kt & 1) * BUF;
    const char* Bb = Ab + ABYTES;
#pragma unroll
    for (int s = 0; s < 2; ++s) {
      bf16x8 af[MT], bfr[NT];
#pragma unroll
      for (int mt = 0; mt < MT; ++mt) { const int row = (wm * MT + mt) * 16 + l15; af[mt] = *(const bf16x8*)(Ab + row * 128 + (((4 * s + g) ^ (row & 7)) * 16)); }
#pragma unroll
      for (int nt = 0; nt < NT; ++nt) { const int c = wn * NT + nt;
        s16x4 lo = trread(Bb + offb(32 * s + 8 * g + (l15 >> 2), 2 * c + ((lane & 3) >> 1)) + 8 * (lane & 1));
        s16x4 hi = trread(Bb + offb(32 * s + 8 * g + 4 + (l15 >> 2), 2 * c + ((lane & 3) >> 1)) + 8 * (lane & 1));
        bfr[nt] = bf16x8{lo[0], lo[1], lo[2], lo[3], hi[0], hi[1], hi[2], hi[3]}; }
#pragma unroll
      for (int nt = 0; nt < NT; ++nt)
#pragma unroll
        for (int mt = 0; mt < MT; ++mt) acc[nt][mt] = __builtin_amdgcn_mfma_f32_16x16x32_bf16(bfr[nt], af[mt], acc[nt][mt], 0, 0, 0);
    }
  }
#pragma unroll
  for (int nt = 0; nt < NT; ++nt)
#pragma unroll
    for (int mt = 0; mt < MT; ++mt) {
      const int m = m0 + (wm * MT + mt) * 16 + l15, n = n0 + (wn * NT + nt) * 16 + 4 * g;
      if constexpr (OUTF32) { *(f32x4*)((float*)Cout + (size_t)blockIdx.z * M * N + (size_t)m * N + n) = acc[nt][mt]; }
      else { u32x2 w = {cvtpk(acc[nt][mt][0], acc[nt][mt][1]), cvtpk(acc[nt][mt][2], acc[nt][mt][3])}; *(u32x2*)((bf16*)Cout + (size_t)m * N + n) = w; }
    }
}

__global__ __launch_bounds__(256) void reduce_kernel(const float* __restrict__ slabs, bf16* __restrict__ out, int n, int nsplit) {
  const int i = (blockIdx.x * 256 + threadIdx.x) * 4;
  if (i >= n) return;
  f32x4 a = *(const f32x4*)(slabs + i);
  for (int s = 1; s < nsplit; ++s) { f32x4 b = *(const f32x4*)(slabs + (size_t)s * n + i); a += b; }
  u32x2 w = {cvtpk(a[0], a[1]), cvtpk(a[2], a[3])};
  *(u32x2*)(out + i) = w;
}

template <int NQT>
__device__ __forceinline__ void attn_body(const bf16* __restrict__ Qh, const bf16* __restrict__ Kg, const bf16* __restrict__ Vg, bf16* __restrict__ Oh,
                                          char* lds, int wid, int lane) {
  const int l15 = lane & 15, g = lane >> 4, sh = wid & 1;
  bf16x8 qf[NQT][4];
#pragma unroll
  for (int qt = 0; qt < NQT; ++qt)
#pragma unroll
    for (int ks = 0; ks < 4; ++ks) qf[qt][ks] = *(const bf16x8*)(Qh + (size_t)(qt * 16 + l15) * QCOLS + ks * 32 + g * 8);
  f32x4 O[8][NQT];
#pragma unroll
  for (int dt = 0; dt < 8; ++dt)
#pragma unroll
    for (int qt = 0; qt < NQT; ++qt) O[dt][qt] = f32x4{0.f, 0.f, 0.f, 0.f};
  float mrow[NQT], lrow[NQT];
#pragma unroll
  for (int qt = 0; qt < NQT; ++qt) { mrow[qt] = -__builtin_inff(); lrow[qt] = 0.f; }

  const int st_ = wid >> 2, c_ = wid & 3, e_ = lane >> 4;
  const unsigned voff = (unsigned)((e_ * DH + ((lane & 15) ^ ((e_ << 2) | c_)) * 8) * 2);
  const char* kbase = (const char*)(Kg + (size_t)(st_ * 512 + 4 * c_) * DH) ;
  const int r3_ = (lane >> 2) & 7;
  const unsigned voffv = (unsigned)((((16 * (r3_ >> 2) + (r3_ & 3)) * DH) + (4 * (2 * (c_ >> 1) + (lane >> 5)) + ((lane & 3) ^ ((2 * (c_ & 1) + (r3_ >> 2)) & 3))) * 8) * 2);
  const char* vbase = (const char*)(Vg + (size_t)(st_ * 512 + 4 * (c_ & 1)) * DH);
  auto stageK = [&](int j, int buf) {
    char* dk = lds + (buf * 2 + st_) * 8192 + c_ * 1024;
    const char* kb = kbase + (size_t)j * 32 * DH * 2 + voff;
#pragma unroll
    for (int i = 0; i < 2; ++i) __builtin_amdgcn_global_load_lds((const unsigned*)(kb + i * 16 * DH * 2), (unsigned*)(dk + i * 4096), 16, 0, 0);
  };
  auto stageV = [&](int j, int buf) {
    char* dv = lds + 32768 + (buf * 2 + st_) * 8192 + c_ * 1024;
#pragma unroll
    for (int i = 0; i < 2; ++i) { const int rho = 4 * (c_ + 4 * i) + e_; const int kap = ((rho >> 2) & 1) * 16 + (rho >> 3) * 4 + (rho & 3);
      const int ch = (lane & 15) ^ ((e_ << 2) | c_);
      __builtin_amdgcn_global_load_lds((const unsigned*)(Vg + (size_t)(st_ * 512 + j * 32 + kap) * DH + ch * 8), (unsigned*)(dv + i * 4096), 16, 0, 0); }
  };
  f32x4 Osum[NQT];
#pragma unroll
  for (int qt = 0; qt < NQT; ++qt) Osum[qt] = f32x4{0.f, 0.f, 0.f, 0.f};
  const short one_bf16 = 0x3F80;
  const bf16x8 ones = bf16x8{one_bf16, one_bf16, one_bf16, one_bf16, one_bf16, one_bf16, one_bf16, one_bf16};
  bf16x8 pf[NQT];
#pragma unroll
  for (int qt = 0; qt < NQT; ++qt) pf[qt] = bf16x8{0, 0, 0, 0, 0, 0, 0, 0};
  constexpr int NSTEP = 16;
  auto pv = [&](const char* Vb) {
#pragma unroll
    for (int dt = 0; dt < 8; ++dt) {
      s16x4 lo = trread(Vb + offb(8 * g + (l15 >> 2), 2 * dt + ((lane & 3) >> 1)) + 8 * (lane & 1));
      s16x4 hi = trread(Vb + offb(8 * g + 4 + (l15 >> 2), 2 * dt + ((lane & 3) >> 1)) + 8 * (lane & 1));
      const bf16x8 vf = bf16x8{lo[0], lo[1], lo[2], lo[3], hi[0], hi[1], hi[2], hi[3]};
#pragma unroll
      for (int qt = 0; qt < NQT; ++qt) O[dt][qt] = __builtin_amdgcn_mfma_f32_16x16x32_bf16(vf, pf[qt], O[dt][qt], 0, 0, 0);
    }
#pragma unroll
    for (int qt = 0; qt < NQT; ++qt) Osum[qt] = __builtin_amdgcn_mfma_f32_16x16x32_bf16(ones, pf[qt], Osum[qt], 0, 0, 0);
  };
  stageK(0, 0);
  for (int j = 0; j < NSTEP; ++j) {
    asm volatile("s_waitcnt vmcnt(0)" ::: "memory");
    __syncthreads(); __builtin_amdgcn_sched_barrier(0);
    if (j + 1 < NSTEP) stageK(j + 1, (j + 1) & 1);
    stageV(j, j & 1);
    const char* Kb = lds + ((j & 1) * 2 + sh) * 8192;
    const char* Vb = lds + 32768 + (((j + 1) & 1) * 2 + sh) * 8192;
    f32x4 S[2][NQT];
#pragma unroll
    for (int t = 0; t < 2; ++t)
#pragma unroll
      for (int qt = 0; qt < NQT; ++qt) S[t][qt] = f32x4{0.f, 0.f, 0.f, 0.f};
#pragma unroll
    for (int t = 0; t < 2; ++t)
#pragma unroll
      for (int ks = 0; ks < 4; ++ks) {
        const bf16x8 kf = *(const bf16x8*)(Kb + offb(16 * t + l15, 4 * ks + g));
#pragma unroll
        for (int qt = 0; qt < NQT; ++qt) S[t][qt] = __builtin_amdgcn_mfma_f32_16x16x32_bf16(kf, qf[qt][ks], S[t][qt], 0, 0, 0);
      }
#pragma unroll
    for (int qt = 0; qt < NQT; ++qt) {
      float mx = fmaxf(fmaxf(S[0][qt][0], S[0][qt][1]), fmaxf(S[0][qt][2], S[0][qt][3]));
      mx = fmaxf(mx, fmaxf(fmaxf(S[1][qt][0], S[1][qt][1]), fmaxf(S[1][qt][2], S[1][qt][3])));
      mx = xor32max(xor16max(mx));
      const float mnew = fmaxf(mrow[qt], mx);
      const float alpha = __builtin_amdgcn_exp2f((mrow[qt] - mnew) * CEXP);
      mrow[qt] = mnew;
      const float nm = -mnew * CEXP;
#pragma unroll
      for (int t = 0; t < 2; ++t)
#pragma unroll
        for (int r = 0; r < 4; ++r) S[t][qt][r] = __builtin_amdgcn_exp2f(fmaf(S[t][qt][r], CEXP, nm));
#pragma unroll
      for (int dt = 0; dt < 8; ++dt) O[dt][qt] *= alpha;
      Osum[qt] *= alpha;
      __attribute__((ext_vector_type(4))) unsigned w = {cvtpk(S[0][qt][0], S[0][qt][1]), cvtpk(S[0][qt][2], S[0][qt][3]),
                                                         cvtpk(S[1][qt][0], S[1][qt][1]), cvtpk(S[1][qt][2], S[1][qt][3])};
      pf[qt] = *reinterpret_cast<bf16x8*>(&w);
    }
    asm volatile("s_waitcnt vmcnt(0)" ::: "memory");
    __syncthreads(); __builtin_amdgcn_sched_barrier(0);
    pv(lds + 32768 + ((j & 1) * 2 + sh) * 8192);
  }
#pragma unroll
  for (int qt = 0; qt < NQT; ++qt) lrow[qt] = Osum[qt][0];
  __syncthreads(); __builtin_amdgcn_sched_barrier(0);
  char* xb = lds + (wid >> 1) * 32768;
  if (sh == 1) {
#pragma unroll
    for (int dt = 0; dt < 8; ++dt)
#pragma unroll
      for (int qt = 0; qt < NQT; ++qt) *(f32x4*)(xb + ((dt * NQT + qt) * 64 + lane) * 16) = O[dt][qt];
#pragma unroll
    for (int qt = 0; qt < NQT; ++qt) { *(float*)(xb + 28672 + (qt * 64 + lane) * 4) = mrow[qt]; *(float*)(xb + 28672 + 1024 + (qt * 64 + lane) * 4) = lrow[qt]; }
  }
  __syncthreads(); __builtin_amdgcn_sched_barrier(0);
  if (sh == 0) {
#pragma unroll
    for (int qt = 0; qt < NQT; ++qt) {
      const float m2 = *(const float*)(xb + 28672 + (qt * 64 + lane) * 4), l2 = *(const float*)(xb + 28672 + 1024 + (qt * 64 + lane) * 4);
      const float mn = fmaxf(mrow[qt], m2);
      const float a1 = __builtin_amdgcn_exp2f((mrow[qt] - mn) * CEXP), a2 = __builtin_amdgcn_exp2f((m2 - mn) * CEXP);
      const float inv = 1.f / (lrow[qt] * a1 + l2 * a2);
      const float w1 = a1 * inv, w2 = a2 * inv;
#pragma unroll
      for (int dt = 0; dt < 8; ++dt) {
        const f32x4 o2 = *(const f32x4*)(xb + ((dt * NQT + qt) * 64 + lane) * 16);
        const f32x4 o = O[dt][qt] * w1 + o2 * w2;
        u32x2 w = {cvtpk(o[0], o[1]), cvtpk(o[2], o[3])};
        *(u32x2*)(Oh + (size_t)(qt * 16 + l15) * QCOLS + dt * 16 + 4 * g) = w;
      }
    }
  }
}

__global__ __launch_bounds__(512, 2) void attn_kernel(const bf16* __restrict__ Qws, const bf16* __restrict__ Kc, const bf16* __restrict__ Vc, bf16* __restrict__ Ows) {
  extern __shared__ __attribute__((aligned(16))) char lds[];
  const int bid = blockIdx.x, xcd = bid & 7, jj = bid >> 3;
  const int grp = xcd * 16 + (jj >> 1), pr = jj & 1;
  const int b = grp >> 3, hkv = grp & 7;
  const int tid = threadIdx.x, wid = __builtin_amdgcn_readfirstlane(tid >> 6), lane = tid & 63;
  const int h = hkv * 4 + pr * 2 + ((wid & 3) >> 1), part = wid >> 2;
  const bf16* Kg = Kc + (size_t)grp * SK * DH;
  const bf16* Vg = Vc + (size_t)grp * SK * DH;
  const size_t qoff = (size_t)(b * SQ + part * 48) * QCOLS + h * DH;
  if (part == 0) attn_body<3>(Qws + qoff, Kg, Vg, Ows + qoff, lds, wid, lane);
  else attn_body<2>(Qws + qoff, Kg, Vg, Ows + qoff, lds, wid, lane);
}

extern "C" void kernel_launch(void* const* d_in, const int* in_sizes, int n_in, void* d_out, int out_size, void* d_ws, size_t ws_size, hipStream_t stream) {
  const bf16* x = (const bf16*)d_in[0];
  const bf16* kc = (const bf16*)d_in[1];
  const bf16* vc = (const bf16*)d_in[2];
  const bf16* wq = (const bf16*)d_in[3];
  const bf16* wo = (const bf16*)d_in[4];
  bf16* out = (bf16*)d_out;
  char* ws = (char*)d_ws;
  bf16* Qws = (bf16*)ws;
  bf16* Ows = (bf16*)(ws + (size_t)MROWS * QCOLS * 2);
  float* slabs = (float*)(ws + (size_t)MROWS * QCOLS * 4);
  constexpr int G1LDS = 2 * (128 * 128 + 64 * 256), G2LDS = 2 * (80 * 128 + 64 * 256), ALDS = 131072;
  hipFuncSetAttribute((const void*)gemm_kernel<128, 2, 2, false>, hipFuncAttributeMaxDynamicSharedMemorySize, G1LDS);
  hipFuncSetAttribute((const void*)gemm_kernel<80, 1, 4, true>, hipFuncAttributeMaxDynamicSharedMemorySize, G2LDS);
  hipFuncSetAttribute((const void*)attn_kernel, hipFuncAttributeMaxDynamicSharedMemorySize, ALDS);
  hipLaunchKernelGGL((gemm_kernel<128, 2, 2, false>), dim3(QCOLS / 128, MROWS / 128, 1), dim3(256), G1LDS, stream, x, wq, (void*)Qws, MROWS, QCOLS, DM, DM);
  hipLaunchKernelGGL(attn_kernel, dim3(256), dim3(512), ALDS, stream, Qws, kc, vc, Ows);
  hipLaunchKernelGGL((gemm_kernel<80, 1, 4, true>), dim3(DM / 128, MROWS / 80, 4), dim3(256), G2LDS, stream, Ows, wo, (void*)slabs, MROWS, DM, QCOLS, QCOLS / 4);
  hipLaunchKernelGGL(reduce_kernel, dim3(MROWS * DM / 4 / 256), dim3(256), 0, stream, slabs, out, MROWS * DM, 4);
}
```
